```python
import torch

def _softmax(x, axis=-1):
    return torch.softmax(x, dim=axis)
def _gelu(x):
    return torch.nn.functional.gelu(x, approximate="tanh")
def _silu(x):
    return torch.nn.functional.silu(x)
def _sigmoid(x):
    return torch.sigmoid(x)
def _layernorm(x, axis=-1, eps=1e-5):
    m = x.mean(dim=axis, keepdim=True)
    v = x.var(dim=axis, keepdim=True, unbiased=False)
    return (x - m) / torch.sqrt(v + eps)
def _scatter_add(tgt, idx, src, axis=0):
    return tgt.clone().index_add_(axis, idx, src)
def _segment_sum(src, seg, n):
    out = torch.zeros((n,) + src.shape[1:], dtype=src.dtype)
    return out.index_add_(0, seg, src)
def _pad(x, pw):
    flat = tuple(v for pair in reversed(pw) for v in pair)
    return torch.nn.functional.pad(x, flat)

kernel_name = 'gated_mix__ssm_conv_s5__dense_activation_relu__prompt'

def setup_inputs():

    _w = (lambda a: (lambda t: t.to(torch.bfloat16) if t.is_floating_point() else t)(torch.as_tensor(a)))
    def _A_setup():
        B, S, D = 1, 2048, 640
        P = D
        Lambda_re = -0.5 * (lambda _x: torch.exp(torch.as_tensor(_x)))((lambda *s: torch.randn(*s, dtype=torch.bfloat16) * 0.8)(P))
        Lambda_im = (lambda *s: torch.randn(*s, dtype=torch.bfloat16) * 0.8)(P) * 1.0
        B_re = (lambda *s: torch.randn(*s, dtype=torch.bfloat16) * 0.8)(P, D) * (D ** -0.5)
        B_im = (lambda *s: torch.randn(*s, dtype=torch.bfloat16) * 0.8)(P, D) * (D ** -0.5)
        C_re = (lambda *s: torch.randn(*s, dtype=torch.bfloat16) * 0.8)(D, P) * (P ** -0.5)
        C_im = (lambda *s: torch.randn(*s, dtype=torch.bfloat16) * 0.8)(D, P) * (P ** -0.5)
        log_dt = (lambda *s: torch.randn(*s, dtype=torch.bfloat16) * 0.8)(P) * 0.3 - 2.0
        Dskip = (lambda *s: torch.randn(*s, dtype=torch.bfloat16) * 0.8)(D)
        return {
            "x": _w((lambda *s: torch.randn(*s, dtype=torch.bfloat16) * 0.05)(B, S, D)),
            "Lambda_re": _w(Lambda_re),
            "Lambda_im": _w(Lambda_im),
            "B_re": _w(B_re),
            "B_im": _w(B_im),
            "C_re": _w(C_re),
            "C_im": _w(C_im),
            "log_dt": _w(log_dt),
            "Dskip": _w(Dskip),
        }
    _A_in = _A_setup()

    def _B_setup():
        B, S, D, Dff = 1, 2048, 640, 1728
        return {
            "x": _w((lambda *s: torch.randn(*s, dtype=torch.bfloat16) * 0.05)(B, S, D)),
            "w1": _w((lambda *s: torch.randn(*s, dtype=torch.bfloat16) * 0.05)(D, Dff)),
            "w2": _w((lambda *s: torch.randn(*s, dtype=torch.bfloat16) * 0.05)(Dff, D)),
        }
    _B_in = _B_setup()

    return {
        "A_x": _A_in["x"],
        "A_Lambda_re": _A_in["Lambda_re"],
        "A_Lambda_im": _A_in["Lambda_im"],
        "A_B_re": _A_in["B_re"],
        "A_B_im": _A_in["B_im"],
        "A_C_re": _A_in["C_re"],
        "A_C_im": _A_in["C_im"],
        "A_log_dt": _A_in["log_dt"],
        "A_Dskip": _A_in["Dskip"],
        "B_x": _B_in["x"],
        "B_w1": _B_in["w1"],
        "B_w2": _B_in["w2"],
        "gate_w": _w((lambda *s: torch.randn(*s, dtype=torch.bfloat16) * 0.05)(640) * 16.0),
    }

def reference(A_x, A_Lambda_re, A_Lambda_im, A_B_re, A_B_im, A_C_re, A_C_im, A_log_dt, A_Dskip, B_x, B_w1, B_w2, gate_w):
    def _A_ref(x, Lambda_re, Lambda_im, B_re, B_im, C_re, C_im, log_dt, Dskip):
        Bsz, L, H = x.shape
        Lam = Lambda_re + 1j * Lambda_im
        Bmat = B_re + 1j * B_im
        Cmat = C_re + 1j * C_im
        dt = torch.exp(log_dt)
        Lam_bar = torch.exp(dt * Lam)
        B_bar = ((Lam_bar - 1.0) / Lam)[:, None] * Bmat
        Bu = torch.einsum("bld,pd->blp", x + 0j, B_bar)
        h = torch.zeros_like(Bu[:, 0, :])
        hs = []
        for t in range(L):
            h = Lam_bar[None, :] * h + Bu[:, t, :]
            hs.append(h)
        hseq = torch.stack(hs, 1)
        y = 2.0 * torch.einsum("blp,dp->bld", hseq, Cmat).real + x * Dskip[None, None, :]
        return y

    def _B_ref(x, w1, w2):
        h = torch.einsum("bsd,df->bsf", x, w1)
        zero = torch.zeros_like(h)
        h = torch.maximum(h, zero)
        return torch.einsum("bsf,fd->bsd", h, w2)

    x = A_x
    g = _sigmoid((x * gate_w).sum(-1, keepdims=True))
    a = _A_ref(x, A_Lambda_re, A_Lambda_im, A_B_re, A_B_im, A_C_re, A_C_im, A_log_dt, A_Dskip)
    b = _B_ref(x, B_w1, B_w2)
    return g * a + (1 - g) * b
```

```cpp
#include <hip/hip_runtime.h>
#include <hip/hip_bf16.h>
#include <stdint.h>
#include <type_traits>


typedef short bf16x8 __attribute__((ext_vector_type(8)));
typedef short bf16x4 __attribute__((ext_vector_type(4)));
typedef float f32x4 __attribute__((ext_vector_type(4)));
typedef float f32x2 __attribute__((ext_vector_type(2)));
typedef __bf16 bf2_t __attribute__((ext_vector_type(2)));
typedef unsigned u32x4 __attribute__((ext_vector_type(4)));
typedef __attribute__((address_space(3))) bf16x4* lds_b4_p;
typedef unsigned short u16;

#define SEQ 2048
#define DM 640
#define NP 640
#define DFF 1728
#define KTOT (2 * NP + DFF)

__device__ __forceinline__ float bf2f(u16 v) { return __uint_as_float(((unsigned)v) << 16); }
__device__ __forceinline__ unsigned pack2(float a, float b) {
  f32x2 v = {a, b};
  bf2_t r = __builtin_convertvector(v, bf2_t);
  return __builtin_bit_cast(unsigned, r);
}
__device__ __forceinline__ float bfr(float a) {
  return __uint_as_float(pack2(a, 0.f) << 16);
}
__device__ __forceinline__ float lo16(unsigned u) { return __uint_as_float(u << 16); }
__device__ __forceinline__ float hi16(unsigned u) { return __uint_as_float(u & 0xffff0000u); }

__device__ __forceinline__ void glds16(const void* g, void* l) {
  __builtin_amdgcn_global_load_lds((const unsigned*)g, (unsigned*)l, 16, 0, 0);
}
#define WAIT_V(n) asm volatile("s_waitcnt vmcnt(" #n ")" ::: "memory")
#define WAIT_L0() asm volatile("s_waitcnt lgkmcnt(0)" ::: "memory")
#define BAR() __builtin_amdgcn_s_barrier()

template <int I, int N, class F>
__device__ __forceinline__ void sfor(F&& f) {
  if constexpr (I < N) {
    f(std::integral_constant<int, I>{});
    sfor<I + 1, N>(f);
  }
}
template <int OFF>
__device__ __forceinline__ void lds_b128(bf16x8& d, unsigned a) {
  asm volatile("ds_read_b128 %0, %1 offset:%2" : "=v"(d) : "v"(a), "i"(OFF));
}
template <int OFF>
__device__ __forceinline__ void lds_tr(bf16x4& d, unsigned a) {
  asm volatile("ds_read_b64_tr_b16 %0, %1 offset:%2" : "=v"(d) : "v"(a), "i"(OFF));
}
__device__ __forceinline__ bf16x8 cat8(bf16x4 a, bf16x4 b) {
  return __builtin_shufflevector(a, b, 0, 1, 2, 3, 4, 5, 6, 7);
}
__device__ __forceinline__ void pin(bf16x8& a) { asm volatile("" : "+v"(a)); }
__device__ __forceinline__ void pin(bf16x4& a) { asm volatile("" : "+v"(a)); }
#define WAIT_L(n) asm volatile("s_waitcnt lgkmcnt(" #n ")" ::: "memory")

__device__ __forceinline__ void ssm_lam(int p, const u16* Lre, const u16* Lim, const u16* ldt,
                                        float& lr, float& li) {
  float dt = bfr(expf(bf2f(ldt[p])));
  float ar = bf2f(Lre[p]), ai = bf2f(Lim[p]);
  float zr = dt * ar, zi = dt * ai;
  float e = expf(zr);
  float s, c;
  sincosf(zi, &s, &c);
  lr = e * c;
  li = e * s;
}
__device__ __forceinline__ void ssm_coef(int p, const u16* Lre, const u16* Lim, float lr, float li,
                                         float& cr, float& ci) {
  float ar = bf2f(Lre[p]), ai = bf2f(Lim[p]);
  float nr = lr - 1.0f, ni = li;
  float den = ar * ar + ai * ai;
  cr = (nr * ar + ni * ai) / den;
  ci = (ni * ar - nr * ai) / den;
}

template <int NL>
__device__ __forceinline__ void code_touch(int t) {
  const char* cp = (const char*)(__builtin_amdgcn_s_getpc() & ~63ull);
  if (t < NL) {
    unsigned v = *(const volatile unsigned*)(cp + t * 64);
    asm volatile("" ::"v"(v));
  }
}
__device__ __forceinline__ f32x2 cmul(f32x2 a, f32x2 b) {
  return (f32x2){a[0] * b[0] - a[1] * b[1], a[0] * b[1] + a[1] * b[0]};
}
__device__ __forceinline__ f32x2 csq(f32x2 a) { return (f32x2){a[0] * a[0] - a[1] * a[1], 2.f * a[0] * a[1]}; }
__device__ __forceinline__ f32x2 shfl_up2(f32x2 v, int d) { return (f32x2){__shfl_up(v[0], d), __shfl_up(v[1], d)}; }

#define K1_BM 128
#define K1_TOUCH 200
#define K2_TOUCH 90
#define K1_NK 10
#define K1_SS 32768
#define K1_SF 53248
#define K1_LDS_B (3 * K1_SF)
#define K2_NST 48
#define WP_CHUNKS (8 * K2_NST * 640)
__device__ __forceinline__ int ximg(int k, int t) {
  const int tb = t >> 4;
  return (k >> 3) * 1024 + tb * 256 + ((((k & 7) ^ (((k >> 3) & 1) << 2) ^ tb)) * 32) + ((t >> 3) & 1) * 16;
}

__global__ __launch_bounds__(768, 1) void k1_gemm_scan(
    const u16* __restrict__ x, const u16* __restrict__ Lre, const u16* __restrict__ Lim,
    const u16* __restrict__ Bre, const u16* __restrict__ Bim, const u16* __restrict__ ldt,
    const u16* __restrict__ w1, const u16* __restrict__ Cre, const u16* __restrict__ Cim,
    const u16* __restrict__ w2, const u16* __restrict__ gw, char* __restrict__ ATP,
    unsigned long long* __restrict__ AG, u32x4* __restrict__ WP, float* __restrict__ G) {
  extern __shared__ __attribute__((aligned(16))) char smem[];
  const int tid = threadIdx.x, lane = tid & 63;
  const int wave = __builtin_amdgcn_readfirstlane(tid >> 6);
  const int bid = blockIdx.x;
  const int xcd = bid & 7, idx = bid >> 3;
  const int mt = (xcd & 1) * 8 + (idx & 7);
  const int g = xcd >> 1, sl = idx >> 3;
  const int nS = g < 2 ? 3 : 2;
  const bool is_ssm = sl < nS;
  const int si = (g < 2 ? 3 * g : 2 * g + 2) + sl;
  const int fi = (g < 2 ? g : 2 * g - 2) + (sl - nS);
  const int t0 = mt * K1_BM;

  if (wave >= 8) {
    const int lw = wave - 8;
    if (is_ssm) {
      const u16* src[8];
#pragma unroll
      for (int j = 0; j < 8; ++j) {
        const int i = lw * 8 + j;
        if (i < 16) {
          int row = 8 * i + (lane >> 3), slot = lane & 7;
          int s = ((row >> 1) & 1) | (((row >> 3) & 3) << 1);
          src[j] = x + (size_t)(t0 + row) * DM + ((slot ^ s) * 8);
        } else {
          int n = 8 * (i - 16) + (lane >> 3), slot = lane & 7;
          int p = si * 64 + (n >> 6) * 32 + ((n >> 5) & 1) * 16 + (n & 15);
          const u16* base = ((n >> 4) & 1) ? Bim : Bre;
          src[j] = base + (size_t)p * DM + ((slot ^ (n & 7)) * 8);
        }
      }
      auto issue = [&](int kt) {
        char* sb = smem + (kt & 3) * K1_SS + lw * 8192;
#pragma unroll
        for (int j = 0; j < 8; ++j) { glds16(src[j], sb + j * 1024); src[j] += 64; }
      };
      issue(0);
      issue(1);
      const int ch = tid - 512;
      f32x4 myp = (f32x4){0.f, 0.f, 0.f, 0.f};
      if (ch < 64) {
        float lr, li, cr, ci;
        ssm_lam(si * 64 + ch, Lre, Lim, ldt, lr, li);
        ssm_coef(si * 64 + ch, Lre, Lim, lr, li, cr, ci);
        myp = (f32x4){lr, li, cr, ci};
      }
      WAIT_V(8);
      BAR();
      issue(2);
#pragma unroll 1
      for (int kt = 0; kt < K1_NK; ++kt) {
        if (kt < K1_NK - 2) WAIT_V(8);
        else WAIT_V(0);
        BAR();
        if (kt + 3 < K1_NK) issue(kt + 3);
      }
      BAR();
      if (ch < 64) ((f32x4*)smem)[ch] = myp;
      WAIT_L0();
      BAR();
      BAR();
      if (ch < 64) {
        const f32x2 lam = (f32x2){myp[0], myp[1]};
        f32x2 l32 = lam;
#pragma unroll
        for (int i = 0; i < 5; ++i) l32 = csq(l32);
        const f32x2* xch = (const f32x2*)(smem + 2048);
        f32x2 a = xch[ch];
#pragma unroll
        for (int w = 1; w < 4; ++w) a = cmul(l32, a) + xch[w * 64 + ch];
        unsigned long long* ag = AG + (size_t)(si * 64 + ch) * 2;
        __hip_atomic_store(ag + (size_t)mt * 1280, (1ull << 32) | __float_as_uint(a[0]), __ATOMIC_RELAXED, __HIP_MEMORY_SCOPE_AGENT);
        __hip_atomic_store(ag + (size_t)mt * 1280 + 1, (1ull << 32) | __float_as_uint(a[1]), __ATOMIC_RELAXED, __HIP_MEMORY_SCOPE_AGENT);
        const f32x2 l128 = csq(csq(l32));
        unsigned long long gr[15], gi[15];
        for (int spin = 0; spin < (1 << 17); ++spin) {
          bool ok = true;
#pragma unroll
          for (int m = 0; m < 15; ++m) {
            if (m < mt) {
              gr[m] = __hip_atomic_load(ag + (size_t)m * 1280, __ATOMIC_RELAXED, __HIP_MEMORY_SCOPE_AGENT);
              gi[m] = __hip_atomic_load(ag + (size_t)m * 1280 + 1, __ATOMIC_RELAXED, __HIP_MEMORY_SCOPE_AGENT);
            }
          }
#pragma unroll
          for (int m = 0; m < 15; ++m)
            if (m < mt) ok = ok && (gr[m] >> 32) == 1ull && (gi[m] >> 32) == 1ull;
          if (ok) break;
          __builtin_amdgcn_s_sleep(2);
        }
        f32x2 c = (f32x2){0.f, 0.f};
#pragma unroll
        for (int m = 0; m < 15; ++m)
          if (m < mt) c = cmul(l128, c) + (f32x2){__uint_as_float((unsigned)gr[m]), __uint_as_float((unsigned)gi[m])};
        ((f32x2*)(smem + 6144))[ch] = c;
      }
      WAIT_L0();
      BAR();
    } else {
      const u16* src[13];
      int stp[13];
#pragma unroll
      for (int j = 0; j < 13; ++j) {
        const int i = lw * 13 + j;
        if (i < 16) {
          int row = 8 * i + (lane >> 3), slot = lane & 7;
          int s = ((row >> 1) & 1) | (((row >> 3) & 3) << 1);
          src[j] = x + (size_t)(t0 + row) * DM + ((slot ^ s) * 8);
          stp[j] = 64;
        } else {
          int o16 = (i - 16) * 64 + lane;
          int k = o16 / 36, c16 = o16 - k * 36;
          int gc = ((((c16 >> 1) ^ ((k >> 3) & 1)) << 1) | (c16 & 1));
          src[j] = w1 + (size_t)k * DFF + fi * 288 + gc * 8;
          stp[j] = 64 * DFF;
        }
      }
      int slot = 0;
      auto issue = [&]() {
        char* sb = smem + slot * K1_SF + lw * 13312;
#pragma unroll
        for (int j = 0; j < 13; ++j) { glds16(src[j], sb + j * 1024); src[j] += stp[j]; }
        slot = slot == 2 ? 0 : slot + 1;
      };
      issue();
      issue();
      WAIT_V(13);
      BAR();
#pragma unroll 1
      for (int kt = 0; kt < K1_NK; ++kt) {
        WAIT_V(0);
        BAR();
        if (kt + 2 < K1_NK) issue();
      }
    }
    return;
  }

  const int wm = wave & 3, wn = wave >> 2;
  code_touch<K1_TOUCH>(tid);
  const int i16 = lane & 15, q = lane >> 4;
  const unsigned lbase = (unsigned)(size_t)smem;
  unsigned offA0;
  {
    int row = wm * 32 + 8 * (i16 >> 2) + (i16 & 3);
    int s = ((row >> 1) & 1) | (((row >> 3) & 3) << 1);
    offA0 = lbase + row * 128 + ((q ^ s) * 16);
  }
  const int tb = t0 + wm * 32 + 8 * q;
  using I0 = std::integral_constant<int, 0>;
  using I1 = std::integral_constant<int, 1>;

  if (is_ssm) {
    const unsigned offBs = lbase + 16384 + (wn * 64 + i16) * 128 + ((q ^ (i16 & 7)) * 16);
    f32x4 acc[2][4];
#pragma unroll
    for (int a = 0; a < 2; ++a)
#pragma unroll
      for (int b = 0; b < 4; ++b) acc[a][b] = (f32x4){0.f, 0.f, 0.f, 0.f};
    bf16x8 af[2][2], bs[2][4];
    auto rd = [&](auto KS, int kt) {
      constexpr int ks = decltype(KS)::value;
      const unsigned sb = (kt & 3) * K1_SS;
      const unsigned aA = sb + (offA0 ^ (ks * 64)), aB = sb + (offBs ^ (ks * 64));
      sfor<0, 2>([&](auto I) { lds_b128<I.value * 512>(af[ks][I.value], aA); });
      sfor<0, 4>([&](auto I) { lds_b128<I.value * 2048>(bs[ks][I.value], aB); });
    };
    auto mm = [&](auto SET) {
      constexpr int S = decltype(SET)::value;
      sfor<0, 2>([&](auto I) { pin(af[S][I.value]); });
      sfor<0, 4>([&](auto I) { pin(bs[S][I.value]); });
#pragma unroll
      for (int rb = 0; rb < 2; ++rb)
#pragma unroll
        for (int cb = 0; cb < 4; ++cb)
          acc[rb][cb] = __builtin_amdgcn_mfma_f32_16x16x32_bf16(af[S][rb], bs[S][cb], acc[rb][cb], 0, 0, 0);
      __builtin_amdgcn_sched_barrier(0);
    };
    BAR();
    rd(I0{}, 0);
#pragma unroll 1
    for (int kt = 0; kt < K1_NK; ++kt) {
      BAR();
      rd(I1{}, kt);
      WAIT_L(6);
      mm(I0{});
      rd(I0{}, kt + 1);
      WAIT_L(6);
      mm(I1{});
    }
    WAIT_L(0);
    sfor<0, 2>([&](auto I) { pin(af[0][I.value]); });
    sfor<0, 4>([&](auto I) { pin(bs[0][I.value]); });

    const f32x4* ptab = (const f32x4*)smem;
    f32x2* xch = (f32x2*)(smem + 2048);
    const f32x2* cin = (const f32x2*)(smem + 6144);
    BAR();
    BAR();
    f32x2 h[2][8], lam[2], l8[2], l16[2], cq[2];
#pragma unroll
    for (int j = 0; j < 2; ++j) {
      const f32x4 pr = ptab[wn * 32 + j * 16 + i16];
      lam[j] = (f32x2){pr[0], pr[1]};
      const f32x2 cf = (f32x2){pr[2], pr[3]};
      f32x2 s = (f32x2){0.f, 0.f};
#pragma unroll
      for (int k = 0; k < 8; ++k) {
        f32x2 a = (f32x2){acc[k >> 2][2 * j][k & 3], acc[k >> 2][2 * j + 1][k & 3]};
        s = cmul(lam[j], s) + cmul(cf, a);
        h[j][k] = s;
      }
      l8[j] = csq(csq(csq(lam[j])));
      l16[j] = csq(l8[j]);
      f32x2 v = s;
      {
        f32x2 u = shfl_up2(v, 16);
        if (q >= 1) v += cmul(l8[j], u);
      }
      {
        f32x2 u = shfl_up2(v, 32);
        if (q >= 2) v += cmul(l16[j], u);
      }
      cq[j] = shfl_up2(v, 16);
      if (q == 0) cq[j] = (f32x2){0.f, 0.f};
      if (q == 3) xch[wm * 64 + wn * 32 + j * 16 + i16] = v;
    }
    WAIT_L0();
    BAR();
    {
    const int sid = mt * 10 + si;
    u32x4 cv[2];
    int cd[2];
#pragma unroll
    for (int it = 0; it < 2; ++it) {
      const int id = (sid + 160 * it) * 512 + tid;
      cd[it] = -1;
      if (id < 20 * 8 * 640) {
        const int sc = id / 640, d = id - sc * 640;
        const int m = sc >> 3, c = sc & 7;
        const int dtile = (d * 205) >> 14, row = d - dtile * 80;
        cv[it] = *(const u32x4*)((c < 4 ? Cre : Cim) + (size_t)d * NP + 32 * m + (c & 3) * 8);
        if (c >= 4) cv[it] ^= (u32x4){0x80008000u, 0x80008000u, 0x80008000u, 0x80008000u};
        cd[it] = (dtile * K2_NST + 26 + m) * 640 + row * 8 + (c ^ (row & 7));
      }
    }
    u32x4 r[8];
    const int wid = sid * 108 + tid;
    const bool wdo = tid < 108;
    const int bk = wid / 80, bd = wid - bk * 80;
    if (wdo) {
#pragma unroll
      for (int e = 0; e < 8; ++e) r[e] = *(const u32x4*)(w2 + (size_t)(8 * bk + e) * DM + 8 * bd);
    }
    u32x4 gx[2][2], gg[2];
    const int gl = lane < 40 ? lane : 0;
    gg[0] = ((const u32x4*)(gw + gl * 16))[0];
    gg[1] = ((const u32x4*)(gw + gl * 16))[1];
#pragma unroll
    for (int i = 0; i < 2; ++i) {
      int rr = si + 10 * (wave + 8 * i);
      rr = rr < 128 ? rr : 127;
      const u32x4* xp = (const u32x4*)(x + (size_t)(t0 + rr) * DM + gl * 16);
      gx[i][0] = xp[0]; gx[i][1] = xp[1];
    }
#pragma unroll
      for (int it = 0; it < 2; ++it)
        if (cd[it] >= 0) WP[cd[it]] = cv[it];
      if (wdo) {
        const int fs = bk >> 3, c = bk & 7;
        const int st = fs < 26 ? fs : 46;
        const int dtile = bd / 10, row0 = 8 * bd - dtile * 80;
#pragma unroll
        for (int dd = 0; dd < 8; ++dd) {
          u32x4 o;
#pragma unroll
          for (int j = 0; j < 4; ++j) {
            const unsigned lo_ = r[2 * j][dd >> 1], hi_ = r[2 * j + 1][dd >> 1];
            o[j] = (dd & 1) ? ((lo_ >> 16) | (hi_ & 0xffff0000u)) : ((lo_ & 0xffffu) | (hi_ << 16));
          }
          const int row = row0 + dd;
          WP[(dtile * K2_NST + st) * 640 + row * 8 + (c ^ (row & 7))] = o;
        }
      }
#pragma unroll
      for (int i = 0; i < 2; ++i) {
        float sg = 0.f;
#pragma unroll
        for (int h2 = 0; h2 < 2; ++h2)
#pragma unroll
          for (int e = 0; e < 4; ++e) {
            sg += bfr(lo16(gx[i][h2][e]) * lo16(gg[h2][e]));
            sg += bfr(hi16(gx[i][h2][e]) * hi16(gg[h2][e]));
          }
        if (lane >= 40) sg = 0.f;
#pragma unroll
        for (int o = 32; o >= 1; o >>= 1) sg += __shfl_xor(sg, o);
        const int rr = si + 10 * (wave + 8 * i);
        if (lane == 0 && rr < 128) G[t0 + rr] = bfr(1.0f / (1.0f + expf(-bfr(sg))));
      }
    }
    BAR();
#pragma unroll
    for (int j = 0; j < 2; ++j) {
      const int ch = wn * 32 + j * 16 + i16;
      const int p = si * 64 + ch;
      const f32x2 l32 = csq(l16[j]);
      f32x2 w = cin[ch];
      for (int ww = 0; ww < wm; ++ww) w = cmul(l32, w) + xch[ww * 64 + ch];
      if (q & 1) w = cmul(l8[j], w);
      if (q & 2) w = cmul(l16[j], w);
      f32x2 c = cq[j] + w;
#pragma unroll
      for (int k = 0; k < 8; ++k) {
        c = cmul(lam[j], c);
        h[j][k] += c;
      }
      char* dst = ATP + (size_t)((tb >> 6) * K2_NST + 26 + (p >> 5)) * 8192 + ximg(p & 31, tb & 63);
      u32x4 o;
      o[0] = pack2(h[j][0][0], h[j][1][0]); o[1] = pack2(h[j][2][0], h[j][3][0]);
      o[2] = pack2(h[j][4][0], h[j][5][0]); o[3] = pack2(h[j][6][0], h[j][7][0]);
      *(u32x4*)dst = o;
      o[0] = pack2(h[j][0][1], h[j][1][1]); o[1] = pack2(h[j][2][1], h[j][3][1]);
      o[2] = pack2(h[j][4][1], h[j][5][1]); o[3] = pack2(h[j][6][1], h[j][7][1]);
      *(u32x4*)(dst + 4096) = o;
    }
  } else {
    const int qq = i16 >> 2, pp = lane & 3;
    const unsigned offB0 = lbase + 16384 + (8 * q + qq) * 576 + pp * 8;
    const unsigned x1 = (q & 1) * 32;
    f32x4 acc[2][9];
#pragma unroll
    for (int a = 0; a < 2; ++a)
#pragma unroll
      for (int b = 0; b < 9; ++b) acc[a][b] = (f32x4){0.f, 0.f, 0.f, 0.f};
    bf16x8 af[2][2];
    bf16x4 lo[2][3], hi[2][3];
    auto rdB = [&](auto S, unsigned sb) {
      constexpr int s = decltype(S)::value, ks = s / 3, grp = s % 3, set = s & 1;
      sfor<0, 3>([&](auto I) {
        constexpr int cb = grp * 3 + I.value;
        const unsigned a = sb + offB0 + ((unsigned)((wn * 9 + cb) * 32) ^ x1);
        lds_tr<ks * 18432>(lo[set][I.value], a);
        lds_tr<ks * 18432 + 2304>(hi[set][I.value], a);
      });
    };
    auto rdA = [&](auto KS, unsigned sb) {
      constexpr int ks = decltype(KS)::value;
      const unsigned aA = sb + (offA0 ^ (ks * 64));
      sfor<0, 2>([&](auto I) { lds_b128<I.value * 512>(af[ks][I.value], aA); });
    };
    auto mm = [&](auto S) {
      constexpr int s = decltype(S)::value, ks = s / 3, grp = s % 3, set = s & 1;
      sfor<0, 2>([&](auto I) { pin(af[ks][I.value]); });
      sfor<0, 3>([&](auto I) { pin(lo[set][I.value]); pin(hi[set][I.value]); });
#pragma unroll
      for (int rb = 0; rb < 2; ++rb)
#pragma unroll
        for (int c = 0; c < 3; ++c)
          acc[rb][grp * 3 + c] = __builtin_amdgcn_mfma_f32_16x16x32_bf16(af[ks][rb], cat8(lo[set][c], hi[set][c]), acc[rb][grp * 3 + c], 0, 0, 0);
      __builtin_amdgcn_sched_barrier(0);
    };
    using I2 = std::integral_constant<int, 2>;
    using I3 = std::integral_constant<int, 3>;
    using I4 = std::integral_constant<int, 4>;
    using I5 = std::integral_constant<int, 5>;
    unsigned sb = 0;
    BAR();
    rdA(I0{}, 0);
    rdB(I0{}, 0);
#pragma unroll 1
    for (int kt = 0; kt < K1_NK; ++kt) {
      BAR();
      const unsigned sbn = sb == 2 * K1_SF ? 0 : sb + K1_SF;
      rdB(I1{}, sb); WAIT_L(6); mm(I0{});
      rdB(I2{}, sb); WAIT_L(6); mm(I1{});
      rdA(I1{}, sb); rdB(I3{}, sb); WAIT_L(8); mm(I2{});
      rdB(I4{}, sb); WAIT_L(6); mm(I3{});
      rdB(I5{}, sb); WAIT_L(6); mm(I4{});
      rdA(I0{}, sbn); rdB(I0{}, sbn); WAIT_L(8); mm(I5{});
      sb = sbn;
    }
    WAIT_L(0);
    sfor<0, 2>([&](auto I) { pin(af[0][I.value]); });
    sfor<0, 3>([&](auto I) { pin(lo[0][I.value]); pin(hi[0][I.value]); });
#pragma unroll
    for (int cb = 0; cb < 9; ++cb) {
      const int f = fi * 288 + wn * 144 + cb * 16 + i16;
      u32x4 o;
#pragma unroll
      for (int h = 0; h < 2; ++h) {
        o[2 * h + 0] = pack2(fmaxf(acc[h][cb][0], 0.f), fmaxf(acc[h][cb][1], 0.f));
        o[2 * h + 1] = pack2(fmaxf(acc[h][cb][2], 0.f), fmaxf(acc[h][cb][3], 0.f));
      }
      const int fs = f >> 6;
      *(u32x4*)(ATP + (size_t)((tb >> 6) * K2_NST + (fs < 26 ? fs : 46)) * 8192 + ximg(f & 63, tb & 63)) = o;
    }
  }
}

#define K2_STAGE_B 18432
#define K2_NSLOT 8
#define K2_TAB_OFF (K2_NSLOT * K2_STAGE_B)
#define K2_LDS_B K2_TAB_OFF

__global__ __launch_bounds__(512, 1) void k2_gemm_out(
    const u16* __restrict__ x, const u16* __restrict__ Dsk, const char* __restrict__ ATP,
    const u32x4* __restrict__ WP, const float* __restrict__ G, float* __restrict__ out) {
  extern __shared__ __attribute__((aligned(16))) char smem[];
  const int tid = threadIdx.x, lane = tid & 63;
  const int wave = __builtin_amdgcn_readfirstlane(tid >> 6);
  const int bid = blockIdx.x;
  const int xcd = bid & 7, idx = bid >> 3;
  const int tt = (xcd & 3) * 8 + (idx & 7);
  const int dtile = (xcd >> 2) * 4 + (idx >> 3);
  const int t0 = tt * 64, d0 = dtile * 80;
  if (wave >= 4) {
    const int lw = wave - 4;
    const char* src[9];
#pragma unroll
    for (int j = 0; j < 9; ++j) {
      const int id = lw * 9 + j;
      const int sp_ = id >= 18, sid = id - 18 * sp_;
      if (sid < 10) src[j] = (const char*)(WP + (size_t)dtile * K2_NST * 640) + sp_ * 10240 + sid * 1024 + lane * 16;
      else src[j] = ATP + (size_t)(tt * K2_NST + sp_) * 8192 + (sid - 10) * 1024 + lane * 16;
    }
    auto issue2 = [&](int pr) {
      char* sb = smem + ((2 * pr) & (K2_NSLOT - 1)) * K2_STAGE_B + lw * 9216;
#pragma unroll
      for (int j = 0; j < 9; ++j) {
        glds16(src[j], sb + j * 1024);
        const int sid = (lw * 9 + j) % 18;
        src[j] += sid < 10 ? 20480 : 16384;
      }
    };
    issue2(0);
    issue2(1);
    WAIT_V(9);
    BAR();
    issue2(2);
    issue2(3);
#pragma unroll 1
    for (int j = 1; j < 24; ++j) {
      if (j < 22) WAIT_V(18);
      else if (j == 22) WAIT_V(9);
      else WAIT_V(0);
      BAR();
      if (j + 3 < 24) issue2(j + 3);
    }
    return;
  }

  const int wt = wave;
  code_touch<K2_TOUCH>(tid);
  const int i16 = lane & 15, q = lane >> 4, qq = i16 >> 2, pp = lane & 3;
  const unsigned lbase = (unsigned)(size_t)smem;
  const unsigned offW = lbase + i16 * 128 + ((q ^ (i16 & 7)) * 16);
  const unsigned offX = lbase + 10240 + q * 1024 + wt * 256 + ((qq ^ ((q & 1) << 2) ^ wt) * 32) + pp * 8;

  const int t = t0 + wt * 16 + i16;
  const float g = G[t];
  uint2 xv[5], dv[5];
#pragma unroll
  for (int db = 0; db < 5; ++db) {
    xv[db] = *(const uint2*)(x + (size_t)t * DM + d0 + db * 16 + 4 * q);
    dv[db] = *(const uint2*)(Dsk + d0 + db * 16 + 4 * q);
  }

  f32x4 accF[5], accS[5];
#pragma unroll
  for (int a = 0; a < 5; ++a) { accF[a] = (f32x4){0.f, 0.f, 0.f, 0.f}; accS[a] = (f32x4){0.f, 0.f, 0.f, 0.f}; }

  bf16x4 xl[2], xh[2];
  bf16x8 wf[2][5];
  auto rdf = [&](auto KS, int s) {
    constexpr int ks = decltype(KS)::value;
    const unsigned sb = (s & (K2_NSLOT - 1)) * K2_STAGE_B;
    const unsigned aX = sb + offX, aW = sb + (offW ^ (ks * 64));
    lds_tr<ks * 4096>(xl[ks], aX);
    lds_tr<ks * 4096>(xh[ks], aX ^ 128);
    sfor<0, 5>([&](auto I) { lds_b128<I.value * 2048>(wf[ks][I.value], aW); });
  };
  auto mmf = [&](auto KS, f32x4 (&acc)[5]) {
    constexpr int ks = decltype(KS)::value;
    pin(xl[ks]); pin(xh[ks]);
    sfor<0, 5>([&](auto I) { pin(wf[ks][I.value]); });
    const bf16x8 xf = cat8(xl[ks], xh[ks]);
#pragma unroll
    for (int db = 0; db < 5; ++db) acc[db] = __builtin_amdgcn_mfma_f32_16x16x32_bf16(wf[ks][db], xf, acc[db], 0, 0, 0);
    __builtin_amdgcn_sched_barrier(0);
  };
  using I0 = std::integral_constant<int, 0>;
  using I1 = std::integral_constant<int, 1>;
  auto pair = [&](f32x4 (&acc)[5], int j) {
    BAR();
    rdf(I0{}, 2 * j);
    rdf(I1{}, 2 * j);
    WAIT_L(7);
    mmf(I0{}, acc);
    rdf(I0{}, 2 * j + 1);
    WAIT_L(7);
    mmf(I1{}, acc);
    rdf(I1{}, 2 * j + 1);
    WAIT_L(7);
    mmf(I0{}, acc);
    WAIT_L(0);
    mmf(I1{}, acc);
  };
#pragma unroll 1
  for (int j = 0; j < 13; ++j) pair(accF, j);
#pragma unroll 1
  for (int j = 13; j < 23; ++j) pair(accS, j);
  BAR();
  rdf(I0{}, 46);
  rdf(I1{}, 46);
  WAIT_L(7);
  mmf(I0{}, accF);
  WAIT_L(0);
  mmf(I1{}, accF);

  const float omg = bfr(1.0f - g);
#pragma unroll
  for (int db = 0; db < 5; ++db) {
    float xs[4] = {lo16(xv[db].x), hi16(xv[db].x), lo16(xv[db].y), hi16(xv[db].y)};
    float ds[4] = {lo16(dv[db].x), hi16(dv[db].x), lo16(dv[db].y), hi16(dv[db].y)};
    f32x4 o;
#pragma unroll
    for (int r = 0; r < 4; ++r) {
      float a = 2.0f * accS[db][r] + bfr(xs[r] * ds[r]);
      float b = bfr(accF[db][r]);
      o[r] = g * a + bfr(omg * b);
    }
    *(f32x4*)(out + (size_t)t * DM + d0 + db * 16 + 4 * q) = o;
  }
}

extern "C" void kernel_launch(void* const* d_in, const int* in_sizes, int n_in, void* d_out,
                              int out_size, void* d_ws, size_t ws_size, hipStream_t stream) {
  const u16* x = (const u16*)d_in[0];
  const u16* Lre = (const u16*)d_in[1];
  const u16* Lim = (const u16*)d_in[2];
  const u16* Bre = (const u16*)d_in[3];
  const u16* Bim = (const u16*)d_in[4];
  const u16* Cre = (const u16*)d_in[5];
  const u16* Cim = (const u16*)d_in[6];
  const u16* ldt = (const u16*)d_in[7];
  const u16* Dsk = (const u16*)d_in[8];
  const u16* w1 = (const u16*)d_in[10];
  const u16* w2 = (const u16*)d_in[11];
  const u16* gw = (const u16*)d_in[12];
  float* out = (float*)d_out;
  char* ATP = (char*)d_ws;
  unsigned long long* AG = (unsigned long long*)((char*)d_ws + (size_t)32 * K2_NST * 8192);
  u32x4* WP = (u32x4*)((char*)AG + 16 * 1280 * 8);
  float* G = (float*)((char*)WP + (size_t)WP_CHUNKS * 16);

  (void)hipFuncSetAttribute((const void*)k1_gemm_scan, hipFuncAttributeMaxDynamicSharedMemorySize, K1_LDS_B);
  (void)hipFuncSetAttribute((const void*)k2_gemm_out, hipFuncAttributeMaxDynamicSharedMemorySize, K2_LDS_B);
  (void)hipMemsetAsync(AG, 0, 16 * 1280 * 8, stream);
  k1_gemm_scan<<<256, 768, K1_LDS_B, stream>>>(x, Lre, Lim, Bre, Bim, ldt, w1, Cre, Cim, w2, gw, ATP, AG, WP, G);
  k2_gemm_out<<<256, 512, K2_LDS_B, stream>>>(x, Dsk, ATP, WP, G, out);
}
```
